```python
import torch

def _softmax(x, axis=-1):
    return torch.softmax(x, dim=axis)
def _gelu(x):
    return torch.nn.functional.gelu(x, approximate="tanh")
def _silu(x):
    return torch.nn.functional.silu(x)
def _sigmoid(x):
    return torch.sigmoid(x)
def _layernorm(x, axis=-1, eps=1e-5):
    m = x.mean(dim=axis, keepdim=True)
    v = x.var(dim=axis, keepdim=True, unbiased=False)
    return (x - m) / torch.sqrt(v + eps)
def _scatter_add(tgt, idx, src, axis=0):
    return tgt.clone().index_add_(axis, idx, src)
def _segment_sum(src, seg, n):
    out = torch.zeros((n,) + src.shape[1:], dtype=src.dtype)
    return out.index_add_(0, seg, src)
def _pad(x, pw):
    flat = tuple(v for pair in reversed(pw) for v in pair)
    return torch.nn.functional.pad(x, flat)

kernel_name = 'interleave_layers__grouped_kv_mqa__sliding_block__prompt'

def setup_inputs():

    _w = (lambda a: (lambda t: t.to(torch.bfloat16) if t.is_floating_point() else t)(torch.as_tensor(a)))
    def _A_setup():
        B, S, D, H = 1, 2048, 640, 32
        Dh = D // H
        return {
            "x": _w((lambda *s: torch.randn(*s, dtype=torch.bfloat16) * 0.05)(B, S, D)),
            "wq": _w((lambda *s: torch.randn(*s, dtype=torch.bfloat16) * 0.05)(D, H * Dh)),
            "wk": _w((lambda *s: torch.randn(*s, dtype=torch.bfloat16) * 0.05)(D, Dh)),
            "wv": _w((lambda *s: torch.randn(*s, dtype=torch.bfloat16) * 0.05)(D, Dh)),
            "wo": _w((lambda *s: torch.randn(*s, dtype=torch.bfloat16) * 0.05)(H * Dh, D)),
        }
    _A_in = _A_setup()

    def _B_setup():
        B, S, D = 1, 2048, 640
        return {
            "x": _w((lambda *s: torch.randn(*s, dtype=torch.bfloat16) * 0.05)(B, S, D)),
            "wqkv": _w((lambda *s: torch.randn(*s, dtype=torch.bfloat16) * 0.05)(D, 3 * D)),
            "wo": _w((lambda *s: torch.randn(*s, dtype=torch.bfloat16) * 0.05)(D, D)),
        }
    _B_in = _B_setup()

    return {
        "A_x": _A_in["x"],
        "A_wq": _A_in["wq"],
        "A_wk": _A_in["wk"],
        "A_wv": _A_in["wv"],
        "A_wo": _A_in["wo"],
        "B_x": _B_in["x"],
        "B_wqkv": _B_in["wqkv"],
        "B_wo": _B_in["wo"],
    }

def reference(A_x, A_wq, A_wk, A_wv, A_wo, B_x, B_wqkv, B_wo):
    def _A_ref(x, wq, wk, wv, wo):
        B, S, D = x.shape
        H = 32
        Dh = D // H
        q = (lambda x, *d: x.permute(*d))((x @ wq).reshape(B, S, H, Dh), 0, 2, 1, 3)
        k = x @ wk
        v = x @ wv
        scores = torch.einsum("bhqd,bkd->bhqk", q, k) * (Dh ** -0.5)
        p = _softmax(scores, axis=-1)
        o = torch.einsum("bhqk,bkd->bhqd", p, v)
        return (lambda x, *d: x.permute(*d))(o, 0, 2, 1, 3).reshape(B, S, H * Dh) @ wo

    def _B_ref(x, wqkv, wo):
        B, S, D = x.shape
        H = 32; Dh = D // H; W = 256
        qkv = (lambda x, *d: x.permute(*d))((x @ wqkv).reshape(B, S, 3, H, Dh), 2, 0, 3, 1, 4)
        q, k, v = qkv[0], qkv[1], qkv[2]
        scores = torch.einsum("bhqd,bhkd->bhqk", q, k) * (Dh ** -0.5)
        idx = torch.arange(S)
        mask = (idx[:, None] - idx[None, :] >= 0) & (idx[:, None] - idx[None, :] < W)
        scores = torch.where(mask[None, None], scores, -1e9)
        p = _softmax(scores, axis=-1)
        o = torch.einsum("bhqk,bhkd->bhqd", p, v)
        return (lambda x, *d: x.permute(*d))(o, 0, 2, 1, 3).reshape(B, S, D) @ wo

    x = A_x
    x = x + _A_ref(_layernorm(x, axis=-1), A_wq, A_wk, A_wv, A_wo)
    x = x + _B_ref(_layernorm(x, axis=-1), B_wqkv, B_wo)
    x = x + _A_ref(_layernorm(x, axis=-1), A_wq, A_wk, A_wv, A_wo)
    x = x + _B_ref(_layernorm(x, axis=-1), B_wqkv, B_wo)
    return x
```

```cpp
#include <hip/hip_runtime.h>
#include <hip/hip_bf16.h>
#include <stdint.h>

constexpr int S = 2048, D = 640, NH = 32, DH = 20, WIN = 256;
constexpr float QSCALE = 0.22360679774997896f * 1.4426950408889634f;
constexpr float THR = 8.f;

typedef short bf16x8 __attribute__((ext_vector_type(8)));
typedef short s16x4 __attribute__((ext_vector_type(4)));
typedef float f32x16 __attribute__((ext_vector_type(16)));
typedef float f32x2 __attribute__((ext_vector_type(2)));
typedef __bf16 bf2 __attribute__((ext_vector_type(2)));
typedef unsigned u32x4 __attribute__((ext_vector_type(4)));
typedef unsigned u32x2 __attribute__((ext_vector_type(2)));
typedef unsigned short u16;
#define DI __device__ __forceinline__
#define MFMA32(a, b, c) __builtin_amdgcn_mfma_f32_32x32x16_bf16((a), (b), (c), 0, 0, 0)

DI unsigned cvtpk(float lo, float hi) { f32x2 a = {lo, hi}; bf2 c = __builtin_convertvector(a, bf2); return __builtin_bit_cast(unsigned, c); }
DI u16 f2bf(float x) { return (u16)(cvtpk(x, 0.f) & 0xffffu); }
DI float bf2f(u16 x) { return __uint_as_float(((unsigned)x) << 16); }
DI float rbf(float x) { return bf2f(f2bf(x)); }
DI int crow(int reg, int h) { return (reg & 3) + 8 * (reg >> 2) + 4 * h; }

typedef __attribute__((address_space(3))) s16x4 lds_s16x4;
DI s16x4 tr_read(const void* p) { return __builtin_amdgcn_ds_read_tr16_b64_v4i16((lds_s16x4*)p); }

constexpr int ASTR = D * 2 + 16;
constexpr int WSTR = 192;
constexpr int KC = 64;
constexpr int GEMM_LDS = 64 * ASTR + 2 * KC * WSTR;

template <bool LN, int EPI>
__global__ __launch_bounds__(256) void gemm_kernel(const u16* __restrict__ A, const u16* __restrict__ W0, const u16* __restrict__ W1,
                                                   const u16* __restrict__ W2, int ldw, const u16* __restrict__ resid,
                                                   u16* __restrict__ out, u16* __restrict__ kimg, u16* __restrict__ vimg) {
    extern __shared__ __attribute__((aligned(16))) char lds[];
    char* As = lds; char* Ws = lds + 64 * ASTR;
    const int tid = threadIdx.x, lane = tid & 63, wid = __builtin_amdgcn_readfirstlane(tid >> 6);
    const int nt = blockIdx.x, m0 = blockIdx.y * 64, n0 = nt * 64;
    const bool special = (EPI == 0) && (nt == 10);

    const int wk_ = tid >> 3, wc_ = (tid & 7) * 8;
    u32x4 wreg0, wreg1;
    auto wload = [&](int kc) {
        if (!special) {
            wreg0 = *(const u32x4*)(W0 + (size_t)(kc * KC + wk_) * ldw + n0 + wc_);
            wreg1 = *(const u32x4*)(W0 + (size_t)(kc * KC + 32 + wk_) * ldw + n0 + wc_);
        } else {
            u16 t0[8], t1[8];
#pragma unroll
            for (int j = 0; j < 8; ++j) {
                const int c = wc_ + j;
                const int k0 = kc * KC + wk_, k1 = k0 + 32;
                t0[j] = c < 20 ? W1[k0 * 20 + c] : (c < 40 ? W2[k0 * 20 + c - 20] : (u16)0);
                t1[j] = c < 20 ? W1[k1 * 20 + c] : (c < 40 ? W2[k1 * 20 + c - 20] : (u16)0);
            }
#pragma unroll
            for (int j = 0; j < 4; ++j) { wreg0[j] = t0[2 * j] | ((unsigned)t0[2 * j + 1] << 16); wreg1[j] = t1[2 * j] | ((unsigned)t1[2 * j + 1] << 16); }
        }
    };
    auto wstore = [&](int buf) {
        *(u32x4*)(Ws + buf * KC * WSTR + wk_ * WSTR + wc_ * 2) = wreg0;
        *(u32x4*)(Ws + buf * KC * WSTR + (32 + wk_) * WSTR + wc_ * 2) = wreg1;
    };
    wload(0);

    {
        const int r = tid >> 2, qd = tid & 3;
        const u16* src = A + (size_t)(m0 + r) * D + qd * 160;
        u32x4 v[20];
#pragma unroll
        for (int i = 0; i < 20; ++i) v[i] = *(const u32x4*)(src + i * 8);
        if (LN) {
            float s = 0.f;
#pragma unroll
            for (int i = 0; i < 20; ++i)
#pragma unroll
                for (int j = 0; j < 4; ++j) s += bf2f((u16)(v[i][j] & 0xffff)) + bf2f((u16)(v[i][j] >> 16));
            s += __shfl_xor(s, 1); s += __shfl_xor(s, 2);
            const float mean = s * (1.f / D);
            float q = 0.f;
#pragma unroll
            for (int i = 0; i < 20; ++i)
#pragma unroll
                for (int j = 0; j < 4; ++j) { float a = bf2f((u16)(v[i][j] & 0xffff)) - mean, b = bf2f((u16)(v[i][j] >> 16)) - mean; q += a * a + b * b; }
            q += __shfl_xor(q, 1); q += __shfl_xor(q, 2);
            const float mb = rbf(mean), vb = rbf(q * (1.f / D));
            const float den = rbf(sqrtf(rbf(vb + 1e-5f)));
            const float inv = 1.f / den;
#pragma unroll
            for (int i = 0; i < 20; ++i)
#pragma unroll
                for (int j = 0; j < 4; ++j) {
                    float a = rbf(bf2f((u16)(v[i][j] & 0xffff)) - mb) * inv, b = rbf(bf2f((u16)(v[i][j] >> 16)) - mb) * inv;
                    v[i][j] = cvtpk(a, b);
                }
        }
#pragma unroll
        for (int i = 0; i < 20; ++i) *(u32x4*)(As + r * ASTR + qd * 320 + i * 16) = v[i];
    }
    wstore(0);
    __syncthreads();

    const int wm = wid & 1, wn = wid >> 1, r32 = lane & 31, h = lane >> 5;
    const int i16 = lane & 15, tq = i16 >> 2, tp = i16 & 3, blk = (lane >> 4) & 1;
    f32x16 acc = {};
    const char* abase = As + (wm * 32 + r32) * ASTR + h * 16;
    const char* wbase = Ws + (8 * h + tq) * WSTR + (wn * 32 + blk * 16 + tp * 4) * 2;
#pragma unroll 1
    for (int kc = 0; kc < D / KC; ++kc) {
        const int buf = kc & 1;
        if (kc + 1 < D / KC) wload(kc + 1);
#pragma unroll
        for (int ks = 0; ks < KC / 16; ++ks) {
            const bf16x8 a = *(const bf16x8*)(abase + (kc * KC + ks * 16) * 2);
            const char* wp = wbase + buf * KC * WSTR + ks * 16 * WSTR;
            s16x4 lo = tr_read(wp), hi = tr_read(wp + 4 * WSTR);
            const bf16x8 b = __builtin_shufflevector(lo, hi, 0, 1, 2, 3, 4, 5, 6, 7);
            acc = MFMA32(a, b, acc);
        }
        if (kc + 1 < D / KC) wstore(buf ^ 1);
        __syncthreads();
    }

    const int col = n0 + wn * 32 + r32;
    if (EPI == 2) {
#pragma unroll
        for (int i = 0; i < 16; ++i) {
            const int row = m0 + wm * 32 + crow(i, h);
            const float v = rbf(acc[i]) + bf2f(resid[(size_t)row * D + col]);
            out[(size_t)row * D + col] = f2bf(v);
        }
    } else if (EPI == 0) {
        if (!special) {
#pragma unroll
            for (int i = 0; i < 16; ++i) out[(size_t)(m0 + wm * 32 + crow(i, h)) * D + col] = f2bf(rbf(acc[i]) * QSCALE);
        } else {
            const int c = wn * 32 + r32;
            if (c < 20) {
#pragma unroll
                for (int i = 0; i < 16; ++i) { const int row = m0 + wm * 32 + crow(i, h);
                    kimg[row * 32 + (((c >> 3) ^ ((row >> 2) & 3)) << 3) + (c & 7)] = f2bf(acc[i]); }
            } else if (c < 40) {
                const int dim = c - 20, t = (m0 >> 5) + wm;
#pragma unroll
                for (int s2 = 0; s2 < 2; ++s2) {
                    u32x4 w;
#pragma unroll
                    for (int j = 0; j < 4; ++j) w[j] = cvtpk(acc[8 * s2 + 2 * j], acc[8 * s2 + 2 * j + 1]);
                    *(u32x4*)(vimg + t * 1024 + dim * 32 + (((2 * s2 + h) ^ ((dim >> 2) & 3)) << 3)) = w;
                }
            }
            for (int e = tid; e < 64 * 12; e += 256) { const int row = m0 + e / 12, c2 = 20 + e % 12;
                kimg[row * 32 + (((c2 >> 3) ^ ((row >> 2) & 3)) << 3) + (c2 & 7)] = 0; }
            for (int e = tid; e < 2 * 12 * 4; e += 256) { const int t = (m0 >> 5) + e / 48, dim = 20 + (e % 48) / 4, ch = e & 3;
                const unsigned val = dim == 20 ? 0x3f803f80u : 0u; u32x4 w = {val, val, val, val};
                *(u32x4*)(vimg + t * 1024 + dim * 32 + ch * 8) = w; }
        }
    } else {
        const int sec = col / D, cc = col - sec * D, hd = cc / DH, dim = cc - hd * DH;
        if (sec == 0) {
#pragma unroll
            for (int i = 0; i < 16; ++i) out[(size_t)(m0 + wm * 32 + crow(i, h)) * D + cc] = f2bf(rbf(acc[i]) * QSCALE);
        } else if (sec == 1) {
            u16* kh = kimg + (size_t)hd * S * 32;
#pragma unroll
            for (int i = 0; i < 16; ++i) { const int row = m0 + wm * 32 + crow(i, h);
                kh[row * 32 + (((dim >> 3) ^ ((row >> 2) & 3)) << 3) + (dim & 7)] = f2bf(acc[i]); }
        } else {
            u16* vh = vimg + (size_t)hd * S * 32;
            const int t = (m0 >> 5) + wm;
#pragma unroll
            for (int s2 = 0; s2 < 2; ++s2) {
                u32x4 w;
#pragma unroll
                for (int j = 0; j < 4; ++j) w[j] = cvtpk(acc[8 * s2 + 2 * j], acc[8 * s2 + 2 * j + 1]);
                *(u32x4*)(vh + t * 1024 + dim * 32 + (((2 * s2 + h) ^ ((dim >> 2) & 3)) << 3)) = w;
            }
        }
    }
}

__global__ __launch_bounds__(256) void padB_kernel(u16* __restrict__ kimg, u16* __restrict__ vimg) {
    const int idx = blockIdx.x * 256 + threadIdx.x;
    const int row = idx & (S - 1);
    u16* kr = kimg + (size_t)idx * 32;
    const int sw = (row >> 2) & 3;
    for (int c = 20; c < 32; ++c) kr[(((c >> 3) ^ sw) << 3) + (c & 7)] = 0;
    const int e = idx & 31;
    if (e < 24) { const int dim = 20 + e / 2; const int half = e & 1;
        const unsigned val = dim == 20 ? 0x3f803f80u : 0u; u32x4 w = {val, val, val, val};
        u16* vr = vimg + (size_t)(idx >> 5) * 1024 + dim * 32 + half * 16;
        *(u32x4*)vr = w; *(u32x4*)(vr + 8) = w; }
}

struct AttnState { f32x16 o, negm; bool first; };

template <bool MASK>
DI void attn_tile(const char* kt, const char* vt, const bf16x8& q1, const bf16x8& q2, f32x16& o, f32x16& negm, bool& first,
                  int r32, int h, int dq  ) {
    const int sw = (r32 >> 2) & 3;
    const bf16x8 k1 = *(const bf16x8*)(kt + r32 * 64 + ((h ^ sw) << 4));
    const bf16x8 k2 = *(const bf16x8*)(kt + r32 * 64 + (((2 + h) ^ sw) << 4));
    const bf16x8 v1 = *(const bf16x8*)(vt + r32 * 64 + ((h ^ sw) << 4));
    const bf16x8 v2 = *(const bf16x8*)(vt + r32 * 64 + (((2 + h) ^ sw) << 4));
    f32x16 s = MFMA32(k1, q1, negm);
    s = MFMA32(k2, q2, s);
    if (MASK) {
#pragma unroll
        for (int i = 0; i < 16; ++i) { const int c = (i & 3) + 8 * (i >> 2); if ((unsigned)(dq - c) >= (unsigned)WIN) s[i] = -__builtin_inff(); }
    }
    float mx = fmaxf(fmaxf(s[0], s[1]), s[2]);
#pragma unroll
    for (int i = 3; i < 15; i += 2) mx = fmaxf(fmaxf(mx, s[i]), s[i + 1]);
    mx = fmaxf(mx, s[15]);
    if (__builtin_expect(first || __any(mx > THR), 0)) {
        float d = fmaxf(mx, __shfl_xor(mx, 32));
        if (MASK) d = fmaxf(d, -1e30f);
        if (!first) d = fmaxf(d, 0.f);
        const float sc = first ? 1.f : __builtin_amdgcn_exp2f(-d);
#pragma unroll
        for (int i = 0; i < 16; ++i) { s[i] -= d; o[i] *= sc; negm[i] -= d; }
        first = false;
    }
    f32x16 p;
#pragma unroll
    for (int i = 0; i < 16; ++i) p[i] = __builtin_amdgcn_exp2f(s[i]);
    u32x4 w0, w1;
#pragma unroll
    for (int j = 0; j < 4; ++j) { w0[j] = cvtpk(p[2 * j], p[2 * j + 1]); w1[j] = cvtpk(p[8 + 2 * j], p[8 + 2 * j + 1]); }
    o = MFMA32(v1, __builtin_bit_cast(bf16x8, w0), o);
    o = MFMA32(v2, __builtin_bit_cast(bf16x8, w1), o);
}

DI void load_q(const u16* q, int row, int hd, int h, bf16x8& q1, bf16x8& q2) {
    const u16* p = q + (size_t)row * D + hd * DH;
    u32x2 a = *(const u32x2*)(p + 8 * h), b = *(const u32x2*)(p + 8 * h + 4);
    u32x4 w1 = {a[0], a[1], b[0], b[1]};
    u32x2 c = *(const u32x2*)(p + 16);
    u32x4 w2 = {h ? 0u : c[0], h ? 0u : c[1], 0u, 0u};
    q1 = __builtin_bit_cast(bf16x8, w1); q2 = __builtin_bit_cast(bf16x8, w2);
}
DI void store_o(u16* o_out, const f32x16& o, int row, int hd, int h) {
    const float l = __shfl(o[8], (threadIdx.x & 31) + 32);
    const float inv = 1.f / l;
    u16* p = o_out + (size_t)row * D + hd * DH + 4 * h;
#pragma unroll
    for (int g = 0; g < 3; ++g) {
        if (g == 2 && h == 1) break;
        u32x2 w = {cvtpk(o[4 * g] * inv, o[4 * g + 1] * inv), cvtpk(o[4 * g + 2] * inv, o[4 * g + 3] * inv)};
        *(u32x2*)(p + 8 * g) = w;
    }
}

constexpr int CHUNK = 256;
__global__ __launch_bounds__(512) void attnA_kernel(const u16* __restrict__ q, const u16* __restrict__ kimg, const u16* __restrict__ vimg,
                                                     u16* __restrict__ o_out) {
    __shared__ __attribute__((aligned(16))) char lds[2 * 2 * CHUNK * 64];
    const int tid = threadIdx.x, lane = tid & 63, wid = __builtin_amdgcn_readfirstlane(tid >> 6), r32 = lane & 31, h = lane >> 5;
    const int hd = blockIdx.x >> 3, row = (blockIdx.x & 7) * 256 + wid * 32 + r32;
    bf16x8 q1, q2; load_q(q, row, hd, h, q1, q2);
    f32x16 o = {}, negm = {}; bool first = true;
    u32x4 st[4];
    auto sload = [&](int c) {
#pragma unroll
        for (int i = 0; i < 2; ++i) { st[i] = *(const u32x4*)((const char*)kimg + c * CHUNK * 64 + i * 8192 + tid * 16);
                                      st[2 + i] = *(const u32x4*)((const char*)vimg + c * CHUNK * 64 + i * 8192 + tid * 16); } };
    auto sstore = [&](int buf) {
#pragma unroll
        for (int i = 0; i < 2; ++i) { *(u32x4*)(lds + buf * 32768 + i * 8192 + tid * 16) = st[i];
                                      *(u32x4*)(lds + buf * 32768 + 16384 + i * 8192 + tid * 16) = st[2 + i]; } };
    sload(0); sstore(0); __syncthreads();
#pragma unroll 1
    for (int c = 0; c < S / CHUNK; ++c) {
        const int buf = c & 1;
        if (c + 1 < S / CHUNK) sload(c + 1);
        const char* kb = lds + buf * 32768; const char* vb = kb + 16384;
#pragma unroll 2
        for (int t = 0; t < CHUNK / 32; ++t) attn_tile<false>(kb + t * 2048, vb + t * 2048, q1, q2, o, negm, first, r32, h, 0);
        if (c + 1 < S / CHUNK) sstore(buf ^ 1);
        __syncthreads();
    }
    store_o(o_out, o, row, hd, h);
}

__global__ __launch_bounds__(512) void attnB_kernel(const u16* __restrict__ q, const u16* __restrict__ kimg, const u16* __restrict__ vimg,
                                                     u16* __restrict__ o_out) {
    __shared__ __attribute__((aligned(16))) char lds[2 * 512 * 64];
    const int tid = threadIdx.x, lane = tid & 63, wid = __builtin_amdgcn_readfirstlane(tid >> 6), r32 = lane & 31, h = lane >> 5;
    const int hd = blockIdx.x >> 3, q0 = (blockIdx.x & 7) * 256, row = q0 + wid * 32 + r32;
    const int kbase = q0 - 256;
    const char* kg = (const char*)(kimg + (size_t)hd * S * 32); const char* vg = (const char*)(vimg + (size_t)hd * S * 32);
#pragma unroll
    for (int i = 0; i < 4; ++i) {
        const int off = i * 8192 + tid * 16;
        if (kbase >= 0 || off >= 16384) {
            *(u32x4*)(lds + off) = *(const u32x4*)(kg + (ptrdiff_t)kbase * 64 + off);
            *(u32x4*)(lds + 32768 + off) = *(const u32x4*)(vg + (ptrdiff_t)kbase * 64 + off);
        }
    }
    bf16x8 q1, q2; load_q(q, row, hd, h, q1, q2);
    f32x16 o = {}, negm = {}; bool first = true;
    __syncthreads();
    const int lo = (kbase >= 0) ? wid : 8;
    for (int lt = wid + 8; lt >= lo; --lt) {
        const int dq = row - (kbase + lt * 32) - 4 * h;
        attn_tile<true>(lds + lt * 2048, lds + 32768 + lt * 2048, q1, q2, o, negm, first, r32, h, dq);
    }
    store_o(o_out, o, row, hd, h);
}

extern "C" void kernel_launch(void* const* d_in, const int* in_sizes, int n_in, void* d_out, int out_size, void* d_ws,
                              size_t ws_size, hipStream_t stream) {
    const u16* Ax = (const u16*)d_in[0]; const u16* Awq = (const u16*)d_in[1]; const u16* Awk = (const u16*)d_in[2];
    const u16* Awv = (const u16*)d_in[3]; const u16* Awo = (const u16*)d_in[4];
    const u16* Bwqkv = (const u16*)d_in[6]; const u16* Bwo = (const u16*)d_in[7];
    char* ws = (char*)d_ws;
    const size_t XB = (size_t)S * D * 2;
    u16* x1 = (u16*)(ws); u16* x2 = (u16*)(ws + XB); u16* x3 = (u16*)(ws + 2 * XB);
    u16* qb = (u16*)(ws + 3 * XB); u16* ob = (u16*)(ws + 4 * XB);
    u16* kA = (u16*)(ws + 5 * XB); u16* vA = kA + S * 32;
    u16* kB = (u16*)(ws + 6 * XB); u16* vB = kB + (size_t)NH * S * 32;
    u16* out = (u16*)d_out;
    {
        hipFuncSetAttribute((const void*)gemm_kernel<true, 0>, hipFuncAttributeMaxDynamicSharedMemorySize, GEMM_LDS);
        hipFuncSetAttribute((const void*)gemm_kernel<true, 1>, hipFuncAttributeMaxDynamicSharedMemorySize, GEMM_LDS);
        hipFuncSetAttribute((const void*)gemm_kernel<false, 2>, hipFuncAttributeMaxDynamicSharedMemorySize, GEMM_LDS);
    }
    padB_kernel<<<NH * S / 256, 256, 0, stream>>>(kB, vB);
    auto layerA = [&](const u16* xin, u16* xout) {
        gemm_kernel<true, 0><<<dim3(11, 32), 256, GEMM_LDS, stream>>>(xin, Awq, Awk, Awv, D, nullptr, qb, kA, vA);
        attnA_kernel<<<256, 512, 0, stream>>>(qb, kA, vA, ob);
        gemm_kernel<false, 2><<<dim3(10, 32), 256, GEMM_LDS, stream>>>(ob, Awo, nullptr, nullptr, D, xin, xout, nullptr, nullptr);
    };
    auto layerB = [&](const u16* xin, u16* xout) {
        gemm_kernel<true, 1><<<dim3(30, 32), 256, GEMM_LDS, stream>>>(xin, Bwqkv, nullptr, nullptr, 3 * D, nullptr, qb, kB, vB);
        attnB_kernel<<<256, 512, 0, stream>>>(qb, kB, vB, ob);
        gemm_kernel<false, 2><<<dim3(10, 32), 256, GEMM_LDS, stream>>>(ob, Bwo, nullptr, nullptr, D, xin, xout, nullptr, nullptr);
    };
    layerA(Ax, x1); layerB(x1, x2); layerA(x2, x3); layerB(x3, out);
}
```

```cpp
#include <hip/hip_runtime.h>
#include <hip/hip_bf16.h>
#include <stdint.h>

constexpr int S = 2048, D = 640, NH = 32, DH = 20, WIN = 256;
constexpr float QSCALE = 0.22360679774997896f * 1.4426950408889634f;
constexpr float THR = 8.f;

typedef short bf16x8 __attribute__((ext_vector_type(8)));
typedef short s16x4 __attribute__((ext_vector_type(4)));
typedef float f32x16 __attribute__((ext_vector_type(16)));
typedef float f32x2 __attribute__((ext_vector_type(2)));
typedef __bf16 bf2 __attribute__((ext_vector_type(2)));
typedef unsigned u32x4 __attribute__((ext_vector_type(4)));
typedef unsigned u32x2 __attribute__((ext_vector_type(2)));
typedef unsigned short u16;
#define DI __device__ __forceinline__
#define MFMA32(a, b, c) __builtin_amdgcn_mfma_f32_32x32x16_bf16((a), (b), (c), 0, 0, 0)

DI unsigned cvtpk(float lo, float hi) { f32x2 a = {lo, hi}; bf2 c = __builtin_convertvector(a, bf2); return __builtin_bit_cast(unsigned, c); }
DI u16 f2bf(float x) { return (u16)(cvtpk(x, 0.f) & 0xffffu); }
DI float bf2f(u16 x) { return __uint_as_float(((unsigned)x) << 16); }
DI float rbf(float x) { return bf2f(f2bf(x)); }
DI int crow(int reg, int h) { return (reg & 3) + 8 * (reg >> 2) + 4 * h; }

typedef __attribute__((address_space(3))) s16x4 lds_s16x4;
DI s16x4 tr_read(const void* p) { return __builtin_amdgcn_ds_read_tr16_b64_v4i16((lds_s16x4*)p); }

constexpr int KC = 64, NCH = D / KC, PF = 3;
constexpr int CSTR = 68;

template <bool LN, int EPI>
__global__ __launch_bounds__(256) void gemm_kernel(const u16* __restrict__ A, const u16* __restrict__ W0, const u16* __restrict__ W1,
                                                   const u16* __restrict__ W2, int ldw, const u16* __restrict__ resid,
                                                   u16* __restrict__ out, u16* __restrict__ kimg, u16* __restrict__ vimg) {
    __shared__ __attribute__((aligned(16))) char lds[2 * 16384 + 1024];
    float* stat = (float*)(lds + 32768);
    const int tid = threadIdx.x, lane = tid & 63, wid = __builtin_amdgcn_readfirstlane(tid >> 6);
    const int nt = blockIdx.x, m0 = blockIdx.y * 64, n0 = nt * 64;
    const bool special = (EPI == 0) && (nt == 10);
    const int sr = tid >> 3, sc = (tid & 7) * 8;
    u32x4 ra[NCH][2], rw[NCH][2];
    const u16* ap = A + (size_t)(m0 + sr) * D + sc;
    auto gload = [&](int c) {
        ra[c][0] = *(const u32x4*)(ap + c * KC); ra[c][1] = *(const u32x4*)(ap + 32 * D + c * KC);
        if (!special) {
            rw[c][0] = *(const u32x4*)(W0 + (size_t)(c * KC + sr) * ldw + n0 + sc);
            rw[c][1] = *(const u32x4*)(W0 + (size_t)(c * KC + 32 + sr) * ldw + n0 + sc);
        } else {
#pragma unroll
            for (int j = 0; j < 4; ++j) {
                unsigned t[2][2];
#pragma unroll
                for (int e = 0; e < 2; ++e) { const int cc = sc + 2 * j + e;
#pragma unroll
                    for (int hh = 0; hh < 2; ++hh) { const int k = c * KC + 32 * hh + sr;
                        t[hh][e] = cc < 20 ? W1[k * 20 + cc] : (cc < 40 ? W2[k * 20 + cc - 20] : (u16)0); } }
                rw[c][0][j] = t[0][0] | (t[0][1] << 16); rw[c][1][j] = t[1][0] | (t[1][1] << 16);
            }
        }
    };
    float s1[2] = {0.f, 0.f}, s2[2] = {0.f, 0.f}, ws[8] = {};
    auto lstore = [&](int c) {
        char* ab = lds + (c & 1) * 16384; char* wb = ab + 8192;
#pragma unroll
        for (int hh = 0; hh < 2; ++hh) { const int r = sr + 32 * hh;
            *(u32x4*)(ab + r * 128 + (((sc >> 3) ^ (r & 7)) << 4)) = ra[c][hh];
            *(u32x4*)(wb + r * 128 + ((sc * 2) ^ (((r >> 1) & 1) << 6))) = rw[c][hh];
            if (LN) {
#pragma unroll
                for (int j = 0; j < 4; ++j) { const float x0 = bf2f((u16)(ra[c][hh][j] & 0xffff)), x1 = bf2f((u16)(ra[c][hh][j] >> 16));
                    s1[hh] += x0 + x1; s2[hh] += x0 * x0 + x1 * x1;
                    ws[2 * j] += bf2f((u16)(rw[c][hh][j] & 0xffff)); ws[2 * j + 1] += bf2f((u16)(rw[c][hh][j] >> 16)); }
            }
        }
    };
#pragma unroll
    for (int c = 0; c < PF; ++c) gload(c);
    lstore(0);
    __syncthreads();
    const int wm = wid & 1, wn = wid >> 1, r32 = lane & 31, h = lane >> 5;
    const int i16 = lane & 15, tq = i16 >> 2, tp = i16 & 3, blk = (lane >> 4) & 1;
    f32x16 acc = {};
    const int arow = wm * 32 + r32;
#pragma unroll
    for (int c = 0; c < NCH; ++c) {
        if (c + PF < NCH) gload(c + PF);
        const char* ab = lds + (c & 1) * 16384; const char* wb = ab + 8192;
#pragma unroll
        for (int ks = 0; ks < KC / 16; ++ks) {
            const bf16x8 a = *(const bf16x8*)(ab + arow * 128 + (((2 * ks + h) ^ (arow & 7)) << 4));
            const int k0 = ks * 16 + 8 * h + tq, colb = (wn * 32 + blk * 16 + tp * 4) * 2;
            s16x4 lo = tr_read(wb + k0 * 128 + (colb ^ (((k0 >> 1) & 1) << 6)));
            s16x4 hi = tr_read(wb + (k0 + 4) * 128 + (colb ^ (((k0 >> 1) & 1) << 6)));
            const bf16x8 b = __builtin_shufflevector(lo, hi, 0, 1, 2, 3, 4, 5, 6, 7);
            acc = MFMA32(a, b, acc);
        }
        if (c + 1 < NCH) lstore(c + 1);
        __syncthreads();
    }
    if (LN) {
        if (tid < 64) stat[128 + tid] = 0.f;
        __syncthreads();
#pragma unroll
        for (int hh = 0; hh < 2; ++hh) {
            float a = s1[hh], b = s2[hh];
#pragma unroll
            for (int o = 1; o < 8; o <<= 1) { a += __shfl_xor(a, o); b += __shfl_xor(b, o); }
            const float mean = a * (1.f / D), var = fmaxf(b * (1.f / D) - mean * mean, 0.f);
            const float den = rbf(sqrtf(rbf(rbf(var) + 1e-5f)));
            if ((tid & 7) == 0) { stat[sr + 32 * hh] = rbf(mean); stat[64 + sr + 32 * hh] = 1.f / den; }
        }
#pragma unroll
        for (int j = 0; j < 8; ++j) { float v = ws[j];
#pragma unroll
            for (int o = 8; o < 64; o <<= 1) v += __shfl_xor(v, o);
            if (lane < 8) atomicAdd(&stat[128 + sc + j], v); }
        __syncthreads();
        const float wsum = stat[128 + wn * 32 + r32];
#pragma unroll
        for (int i = 0; i < 16; ++i) { const int r = wm * 32 + crow(i, h); acc[i] = (acc[i] - stat[r] * wsum) * stat[64 + r]; }
    }
    const int col = n0 + wn * 32 + r32;
    bool viaLds = true;
    if (EPI == 0 && special) viaLds = false;
    if (EPI == 1 && n0 >= D) viaLds = false;
    if (viaLds) {
        float* ct = (float*)lds;
#pragma unroll
        for (int i = 0; i < 16; ++i) ct[(wm * 32 + crow(i, h)) * CSTR + wn * 32 + r32] = acc[i];
        __syncthreads();
#pragma unroll
        for (int hh = 0; hh < 2; ++hh) { const int r = sr + 32 * hh;
            const float* cp = ct + r * CSTR + sc; float v[8];
#pragma unroll
            for (int j = 0; j < 8; ++j) v[j] = rbf(cp[j]);
            const size_t go = (size_t)(m0 + r) * D + n0 + sc;
            u32x4 w;
            if (EPI == 2) { const u32x4 rr = *(const u32x4*)(resid + go);
#pragma unroll
                for (int j = 0; j < 4; ++j) w[j] = cvtpk(v[2 * j] + bf2f((u16)(rr[j] & 0xffff)), v[2 * j + 1] + bf2f((u16)(rr[j] >> 16)));
            } else {
#pragma unroll
                for (int j = 0; j < 4; ++j) w[j] = cvtpk(v[2 * j] * QSCALE, v[2 * j + 1] * QSCALE);
            }
            *(u32x4*)(out + go) = w;
        }
    } else if (EPI == 0) {
        const int c = wn * 32 + r32;
        if (c < 20) {
#pragma unroll
            for (int i = 0; i < 16; ++i) { const int row = m0 + wm * 32 + crow(i, h);
                kimg[row * 32 + (((c >> 3) ^ ((row >> 2) & 3)) << 3) + (c & 7)] = f2bf(acc[i]); }
        } else if (c < 40) {
            const int dim = c - 20, t = (m0 >> 5) + wm;
#pragma unroll
            for (int q2 = 0; q2 < 2; ++q2) { u32x4 w;
#pragma unroll
                for (int j = 0; j < 4; ++j) w[j] = cvtpk(acc[8 * q2 + 2 * j], acc[8 * q2 + 2 * j + 1]);
                *(u32x4*)(vimg + t * 1024 + dim * 32 + (((2 * q2 + h) ^ ((dim >> 2) & 3)) << 3)) = w; }
        }
        for (int e = tid; e < 64 * 12; e += 256) { const int row = m0 + e / 12, c2 = 20 + e % 12;
            kimg[row * 32 + (((c2 >> 3) ^ ((row >> 2) & 3)) << 3) + (c2 & 7)] = 0; }
        for (int e = tid; e < 2 * 12 * 4; e += 256) { const int t = (m0 >> 5) + e / 48, dim = 20 + (e % 48) / 4, ch = e & 3;
            const unsigned val = dim == 20 ? 0x3f803f80u : 0u; u32x4 w = {val, val, val, val};
            *(u32x4*)(vimg + t * 1024 + dim * 32 + ch * 8) = w; }
    } else if (EPI == 1) {
        const int sec = col / D, cc = col - sec * D, hd = cc / DH, dim = cc - hd * DH;
        if (sec == 1) {
            u16* kh = kimg + (size_t)hd * S * 32;
#pragma unroll
            for (int i = 0; i < 16; ++i) { const int row = m0 + wm * 32 + crow(i, h);
                kh[row * 32 + (((dim >> 3) ^ ((row >> 2) & 3)) << 3) + (dim & 7)] = f2bf(acc[i]); }
            if (dim == 19) {
#pragma unroll
                for (int i = 0; i < 16; ++i) { const int row = m0 + wm * 32 + crow(i, h); const int sw = (row >> 2) & 3;
                    *(u32x2*)(kh + row * 32 + ((2 ^ sw) << 3) + 4) = u32x2{0u, 0u};
                    *(u32x4*)(kh + row * 32 + ((3 ^ sw) << 3)) = u32x4{0u, 0u, 0u, 0u}; }
            }
        } else {
            u16* vh = vimg + (size_t)hd * S * 32;
            const int t = (m0 >> 5) + wm;
#pragma unroll
            for (int q2 = 0; q2 < 2; ++q2) { u32x4 w;
#pragma unroll
                for (int j = 0; j < 4; ++j) w[j] = cvtpk(acc[8 * q2 + 2 * j], acc[8 * q2 + 2 * j + 1]);
                *(u32x4*)(vh + t * 1024 + dim * 32 + (((2 * q2 + h) ^ ((dim >> 2) & 3)) << 3)) = w; }
            if (dim == 19) {
#pragma unroll
                for (int d2 = 20; d2 < 32; ++d2) { const unsigned val = d2 == 20 ? 0x3f803f80u : 0u; u32x4 w = {val, val, val, val};
                    *(u32x4*)(vh + t * 1024 + d2 * 32 + 16 * h) = w; *(u32x4*)(vh + t * 1024 + d2 * 32 + 16 * h + 8) = w; }
            }
        }
    }
}

struct AttnState { f32x16 o, negm; bool first; };

template <bool MASK>
DI void attn_tile(const char* kt, const char* vt, const bf16x8& q1, const bf16x8& q2, f32x16& o, f32x16& negm, bool& first,
                  int r32, int h, int dq  ) {
    const int sw = (r32 >> 2) & 3;
    const bf16x8 k1 = *(const bf16x8*)(kt + r32 * 64 + ((h ^ sw) << 4));
    const bf16x8 k2 = *(const bf16x8*)(kt + r32 * 64 + (((2 + h) ^ sw) << 4));
    const bf16x8 v1 = *(const bf16x8*)(vt + r32 * 64 + ((h ^ sw) << 4));
    const bf16x8 v2 = *(const bf16x8*)(vt + r32 * 64 + (((2 + h) ^ sw) << 4));
    f32x16 s = MFMA32(k1, q1, negm);
    s = MFMA32(k2, q2, s);
    if (MASK) {
#pragma unroll
        for (int i = 0; i < 16; ++i) { const int c = (i & 3) + 8 * (i >> 2); if ((unsigned)(dq - c) >= (unsigned)WIN) s[i] = -__builtin_inff(); }
    }
    float mx = fmaxf(fmaxf(s[0], s[1]), s[2]);
#pragma unroll
    for (int i = 3; i < 15; i += 2) mx = fmaxf(fmaxf(mx, s[i]), s[i + 1]);
    mx = fmaxf(mx, s[15]);
    if (__builtin_expect(first || __any(mx > THR), 0)) {
        float d = fmaxf(mx, __shfl_xor(mx, 32));
        if (MASK) d = fmaxf(d, -1e30f);
        if (!first) d = fmaxf(d, 0.f);
        const float sc = first ? 1.f : __builtin_amdgcn_exp2f(-d);
#pragma unroll
        for (int i = 0; i < 16; ++i) { s[i] -= d; o[i] *= sc; negm[i] -= d; }
        first = false;
    }
    f32x16 p;
#pragma unroll
    for (int i = 0; i < 16; ++i) p[i] = __builtin_amdgcn_exp2f(s[i]);
    u32x4 w0, w1;
#pragma unroll
    for (int j = 0; j < 4; ++j) { w0[j] = cvtpk(p[2 * j], p[2 * j + 1]); w1[j] = cvtpk(p[8 + 2 * j], p[8 + 2 * j + 1]); }
    o = MFMA32(v1, __builtin_bit_cast(bf16x8, w0), o);
    o = MFMA32(v2, __builtin_bit_cast(bf16x8, w1), o);
}

DI void load_q(const u16* q, int row, int hd, int h, bf16x8& q1, bf16x8& q2) {
    const u16* p = q + (size_t)row * D + hd * DH;
    u32x2 a = *(const u32x2*)(p + 8 * h), b = *(const u32x2*)(p + 8 * h + 4);
    u32x4 w1 = {a[0], a[1], b[0], b[1]};
    u32x2 c = *(const u32x2*)(p + 16);
    u32x4 w2 = {h ? 0u : c[0], h ? 0u : c[1], 0u, 0u};
    q1 = __builtin_bit_cast(bf16x8, w1); q2 = __builtin_bit_cast(bf16x8, w2);
}
DI void store_o(u16* o_out, const f32x16& o, int row, int hd, int h) {
    const float l = __shfl(o[8], (threadIdx.x & 31) + 32);
    const float inv = 1.f / l;
    u16* p = o_out + (size_t)row * D + hd * DH + 4 * h;
#pragma unroll
    for (int g = 0; g < 3; ++g) {
        if (g == 2 && h == 1) break;
        u32x2 w = {cvtpk(o[4 * g] * inv, o[4 * g + 1] * inv), cvtpk(o[4 * g + 2] * inv, o[4 * g + 3] * inv)};
        *(u32x2*)(p + 8 * g) = w;
    }
}

constexpr int CHUNK = 128;
__global__ __launch_bounds__(512, 4) void attnA_kernel(const u16* __restrict__ q, const u16* __restrict__ kimg, const u16* __restrict__ vimg,
                                                        u16* __restrict__ o_out) {
    __shared__ __attribute__((aligned(16))) char lds[2 * 32768];
    const int tid = threadIdx.x, lane = tid & 63, wid = __builtin_amdgcn_readfirstlane(tid >> 6), r32 = lane & 31, h = lane >> 5;
    const int hd = blockIdx.x >> 4, half = wid >> 2, row = (blockIdx.x & 15) * 128 + (wid & 3) * 32 + r32;
    bf16x8 q1, q2; load_q(q, row, hd, h, q1, q2);
    f32x16 o = {}, negm = {}; bool first = true;
    u32x4 st[4];
    auto sload = [&](int c) {
#pragma unroll
        for (int i = 0; i < 2; ++i) { const int off = (i * 1024 + c * CHUNK) * 64 + tid * 16;
            st[2 * i] = *(const u32x4*)((const char*)kimg + off); st[2 * i + 1] = *(const u32x4*)((const char*)vimg + off); } };
    auto sstore = [&](int buf) {
#pragma unroll
        for (int i = 0; i < 2; ++i) { *(u32x4*)(lds + buf * 32768 + i * 16384 + tid * 16) = st[2 * i];
                                      *(u32x4*)(lds + buf * 32768 + i * 16384 + 8192 + tid * 16) = st[2 * i + 1]; } };
    sload(0); sstore(0); __syncthreads();
#pragma unroll 1
    for (int c = 0; c < 1024 / CHUNK; ++c) {
        const int buf = c & 1;
        if (c + 1 < 1024 / CHUNK) sload(c + 1);
        const char* kb = lds + buf * 32768 + half * 16384; const char* vb = kb + 8192;
#pragma unroll
        for (int t = 0; t < CHUNK / 32; ++t) attn_tile<false>(kb + t * 2048, vb + t * 2048, q1, q2, o, negm, first, r32, h, 0);
        if (c + 1 < 1024 / CHUNK) sstore(buf ^ 1);
        __syncthreads();
    }
    float* mb = (float*)lds + (wid & 3) * (17 * 64);
    if (half == 1) {
#pragma unroll
        for (int i = 0; i < 16; ++i) mb[i * 64 + lane] = o[i];
        mb[16 * 64 + lane] = negm[0];
    }
    __syncthreads();
    if (half == 0) {
        const float nm1 = mb[16 * 64 + lane], nm0 = negm[0];
        const float nm = fminf(nm0, nm1);
        const float f0 = __builtin_amdgcn_exp2f(nm - nm0), f1 = __builtin_amdgcn_exp2f(nm - nm1);
#pragma unroll
        for (int i = 0; i < 16; ++i) o[i] = o[i] * f0 + mb[i * 64 + lane] * f1;
        store_o(o_out, o, row, hd, h);
    }
}

__global__ __launch_bounds__(256) void attnB_kernel(const u16* __restrict__ q, const u16* __restrict__ kimg, const u16* __restrict__ vimg,
                                                     u16* __restrict__ o_out) {
    __shared__ __attribute__((aligned(16))) char lds[2 * 384 * 64];
    const int tid = threadIdx.x, lane = tid & 63, wid = __builtin_amdgcn_readfirstlane(tid >> 6), r32 = lane & 31, h = lane >> 5;
    const int hd = blockIdx.x >> 4, q0 = (blockIdx.x & 15) * 128, row = q0 + wid * 32 + r32;
    const int kbase = q0 - 256;
    const char* kg = (const char*)(kimg + (size_t)hd * S * 32); const char* vg = (const char*)(vimg + (size_t)hd * S * 32);
    u32x4 st[12];
#pragma unroll
    for (int i = 0; i < 6; ++i) {
        const int off = i * 4096 + tid * 16;
        const bool ok = kbase * 64 + off >= 0;
        st[2 * i] = ok ? *(const u32x4*)(kg + (ptrdiff_t)kbase * 64 + off) : u32x4{0u, 0u, 0u, 0u};
        st[2 * i + 1] = ok ? *(const u32x4*)(vg + (ptrdiff_t)kbase * 64 + off) : u32x4{0u, 0u, 0u, 0u};
    }
    bf16x8 q1, q2; load_q(q, row, hd, h, q1, q2);
#pragma unroll
    for (int i = 0; i < 6; ++i) { const int off = i * 4096 + tid * 16;
        *(u32x4*)(lds + off) = st[2 * i]; *(u32x4*)(lds + 24576 + off) = st[2 * i + 1]; }
    f32x16 o = {}, negm = {}; bool first = true;
    __syncthreads();
    const int lo = max(wid, (-kbase) >> 5);
    {
        const int lt = wid + 8;
        attn_tile<true>(lds + lt * 2048, lds + 24576 + lt * 2048, q1, q2, o, negm, first, r32, h, row - (kbase + lt * 32) - 4 * h);
    }
#pragma unroll 1
    for (int lt = wid + 7; lt > wid && lt >= lo; --lt)
        attn_tile<false>(lds + lt * 2048, lds + 24576 + lt * 2048, q1, q2, o, negm, first, r32, h, 0);
    if (wid >= lo)
        attn_tile<true>(lds + wid * 2048, lds + 24576 + wid * 2048, q1, q2, o, negm, first, r32, h, row - (kbase + wid * 32) - 4 * h);
    store_o(o_out, o, row, hd, h);
}

extern "C" void kernel_launch(void* const* d_in, const int* in_sizes, int n_in, void* d_out, int out_size, void* d_ws,
                              size_t ws_size, hipStream_t stream) {
    const u16* Ax = (const u16*)d_in[0]; const u16* Awq = (const u16*)d_in[1]; const u16* Awk = (const u16*)d_in[2];
    const u16* Awv = (const u16*)d_in[3]; const u16* Awo = (const u16*)d_in[4];
    const u16* Bwqkv = (const u16*)d_in[6]; const u16* Bwo = (const u16*)d_in[7];
    char* ws = (char*)d_ws;
    const size_t XB = (size_t)S * D * 2;
    u16* x1 = (u16*)(ws); u16* x2 = (u16*)(ws + XB); u16* x3 = (u16*)(ws + 2 * XB);
    u16* qb = (u16*)(ws + 3 * XB); u16* ob = (u16*)(ws + 4 * XB);
    u16* kA = (u16*)(ws + 5 * XB); u16* vA = kA + S * 32;
    u16* kB = (u16*)(ws + 6 * XB); u16* vB = kB + (size_t)NH * S * 32;
    u16* out = (u16*)d_out;
    auto layerA = [&](const u16* xin, u16* xout) {
        gemm_kernel<true, 0><<<dim3(11, 32), 256, 0, stream>>>(xin, Awq, Awk, Awv, D, nullptr, qb, kA, vA);
        attnA_kernel<<<512, 512, 0, stream>>>(qb, kA, vA, ob);
        gemm_kernel<false, 2><<<dim3(10, 32), 256, 0, stream>>>(ob, Awo, nullptr, nullptr, D, xin, xout, nullptr, nullptr);
    };
    auto layerB = [&](const u16* xin, u16* xout) {
        gemm_kernel<true, 1><<<dim3(30, 32), 256, 0, stream>>>(xin, Bwqkv, nullptr, nullptr, 3 * D, nullptr, qb, kB, vB);
        attnB_kernel<<<512, 256, 0, stream>>>(qb, kB, vB, ob);
        gemm_kernel<false, 2><<<dim3(10, 32), 256, 0, stream>>>(ob, Bwo, nullptr, nullptr, D, xin, xout, nullptr, nullptr);
    };
    layerA(Ax, x1); layerB(x1, x2); layerA(x2, x3); layerB(x3, out);
}
```

```cpp
#include <hip/hip_runtime.h>
#include <hip/hip_bf16.h>
#include <stdint.h>

constexpr int S = 2048, D = 640, NH = 32, DH = 20, WIN = 256;
constexpr float QSCALE = 0.22360679774997896f * 1.4426950408889634f;
constexpr float THR = 8.f;

typedef short bf16x8 __attribute__((ext_vector_type(8)));
typedef short s16x4 __attribute__((ext_vector_type(4)));
typedef float f32x16 __attribute__((ext_vector_type(16)));
typedef float f32x2 __attribute__((ext_vector_type(2)));
typedef __bf16 bf2 __attribute__((ext_vector_type(2)));
typedef unsigned u32x4 __attribute__((ext_vector_type(4)));
typedef unsigned u32x2 __attribute__((ext_vector_type(2)));
typedef unsigned short u16;
#define DI __device__ __forceinline__
#define MFMA32(a, b, c) __builtin_amdgcn_mfma_f32_32x32x16_bf16((a), (b), (c), 0, 0, 0)

DI unsigned cvtpk(float lo, float hi) { f32x2 a = {lo, hi}; bf2 c = __builtin_convertvector(a, bf2); return __builtin_bit_cast(unsigned, c); }
DI u16 f2bf(float x) { return (u16)(cvtpk(x, 0.f) & 0xffffu); }
DI float bf2f(u16 x) { return __uint_as_float(((unsigned)x) << 16); }
DI float rbf(float x) { return bf2f(f2bf(x)); }
DI int crow(int reg, int h) { return (reg & 3) + 8 * (reg >> 2) + 4 * h; }

typedef __attribute__((address_space(3))) s16x4 lds_s16x4;
DI s16x4 tr_read(const void* p) { return __builtin_amdgcn_ds_read_tr16_b64_v4i16((lds_s16x4*)p); }

constexpr int KC = 64, NCH = D / KC, PF = 4;
constexpr int CSTR = 68;

template <bool LN, int EPI>
__global__ __launch_bounds__(256) void gemm_kernel(const u16* __restrict__ A, const u16* __restrict__ W0, const u16* __restrict__ W1,
                                                   const u16* __restrict__ W2, int ldw, const u16* __restrict__ resid,
                                                   u16* __restrict__ out, u16* __restrict__ kimg, u16* __restrict__ vimg) {
    __shared__ __attribute__((aligned(16))) char lds[4 * 16384 + 1024];
    float* stat = (float*)(lds + 65536);
    const int tid = threadIdx.x, lane = tid & 63, wid = __builtin_amdgcn_readfirstlane(tid >> 6);
    const int nt = blockIdx.x, m0 = blockIdx.y * 64, n0 = nt * 64;
    const bool special = (EPI == 0) && (nt == 10);
    const int sr = tid >> 3, sc = (tid & 7) * 8;
    u32x4 ra[NCH][2], rw[NCH][2];
    const u16* ap = A + (size_t)(m0 + sr) * D + sc;
    auto gload = [&](int c) {
        ra[c][0] = *(const u32x4*)(ap + c * KC); ra[c][1] = *(const u32x4*)(ap + 32 * D + c * KC);
        const u16* wp = special ? W1 : W0 + n0; const int lw = special ? 64 : ldw;
        rw[c][0] = *(const u32x4*)(wp + (size_t)(c * KC + sr) * lw + sc);
        rw[c][1] = *(const u32x4*)(wp + (size_t)(c * KC + 32 + sr) * lw + sc);
    };
    float s1[2] = {0.f, 0.f}, s2[2] = {0.f, 0.f}, ws[8] = {};
    auto lstore = [&](int c) {
        char* ab = lds + (c & 3) * 16384; char* wb = ab + 8192;
#pragma unroll
        for (int hh = 0; hh < 2; ++hh) { const int r = sr + 32 * hh;
            *(u32x4*)(ab + r * 128 + (((sc >> 3) ^ (r & 7)) << 4)) = ra[c][hh];
            *(u32x4*)(wb + r * 128 + ((sc * 2) ^ (((r >> 1) & 1) << 6))) = rw[c][hh];
            if (LN) {
#pragma unroll
                for (int j = 0; j < 4; ++j) { const float x0 = bf2f((u16)(ra[c][hh][j] & 0xffff)), x1 = bf2f((u16)(ra[c][hh][j] >> 16));
                    s1[hh] += x0 + x1; s2[hh] += x0 * x0 + x1 * x1;
                    ws[2 * j] += bf2f((u16)(rw[c][hh][j] & 0xffff)); ws[2 * j + 1] += bf2f((u16)(rw[c][hh][j] >> 16)); }
            }
        }
    };
#pragma unroll
    for (int c = 0; c < PF; ++c) gload(c);
    lstore(0); lstore(1);
    __syncthreads();
    const int wm = wid & 1, wn = wid >> 1, r32 = lane & 31, h = lane >> 5;
    const int i16 = lane & 15, tq = i16 >> 2, tp = i16 & 3, blk = (lane >> 4) & 1;
    f32x16 acc = {};
    const int arow = wm * 32 + r32;
#pragma unroll
    for (int c2 = 0; c2 < NCH; c2 += 2) {
        if (c2 + PF < NCH) gload(c2 + PF);
        if (c2 + PF + 1 < NCH) gload(c2 + PF + 1);
#pragma unroll
        for (int c = c2; c < c2 + 2; ++c) {
            const char* ab = lds + (c & 3) * 16384; const char* wb = ab + 8192;
#pragma unroll
            for (int ks = 0; ks < KC / 16; ++ks) {
                const bf16x8 a = *(const bf16x8*)(ab + arow * 128 + (((2 * ks + h) ^ (arow & 7)) << 4));
                const int k0 = ks * 16 + 8 * h + tq, colb = (wn * 32 + blk * 16 + tp * 4) * 2;
                s16x4 lo = tr_read(wb + k0 * 128 + (colb ^ (((k0 >> 1) & 1) << 6)));
                s16x4 hi = tr_read(wb + (k0 + 4) * 128 + (colb ^ (((k0 >> 1) & 1) << 6)));
                const bf16x8 b = __builtin_shufflevector(lo, hi, 0, 1, 2, 3, 4, 5, 6, 7);
                acc = MFMA32(a, b, acc);
            }
        }
        if (c2 + 2 < NCH) { lstore(c2 + 2); lstore(c2 + 3); }
        __syncthreads();
    }
    if (LN) {
        if (tid < 64) stat[128 + tid] = 0.f;
        __syncthreads();
#pragma unroll
        for (int hh = 0; hh < 2; ++hh) {
            float a = s1[hh], b = s2[hh];
#pragma unroll
            for (int o = 1; o < 8; o <<= 1) { a += __shfl_xor(a, o); b += __shfl_xor(b, o); }
            const float mean = a * (1.f / D), var = fmaxf(b * (1.f / D) - mean * mean, 0.f);
            const float den = rbf(sqrtf(rbf(rbf(var) + 1e-5f)));
            if ((tid & 7) == 0) { stat[sr + 32 * hh] = rbf(mean); stat[64 + sr + 32 * hh] = 1.f / den; }
        }
#pragma unroll
        for (int j = 0; j < 8; ++j) { float v = ws[j];
#pragma unroll
            for (int o = 8; o < 64; o <<= 1) v += __shfl_xor(v, o);
            if (lane < 8) atomicAdd(&stat[128 + sc + j], v); }
        __syncthreads();
        const float wsum = stat[128 + wn * 32 + r32];
#pragma unroll
        for (int i = 0; i < 16; ++i) { const int r = wm * 32 + crow(i, h); acc[i] = (acc[i] - stat[r] * wsum) * stat[64 + r]; }
    }
    const int col = n0 + wn * 32 + r32;
    bool viaLds = true;
    if (EPI == 0 && special) viaLds = false;
    if (EPI == 1 && n0 >= D) viaLds = false;
    if (viaLds) {
        float* ct = (float*)lds;
#pragma unroll
        for (int i = 0; i < 16; ++i) ct[(wm * 32 + crow(i, h)) * CSTR + wn * 32 + r32] = acc[i];
        __syncthreads();
#pragma unroll
        for (int hh = 0; hh < 2; ++hh) { const int r = sr + 32 * hh;
            const float* cp = ct + r * CSTR + sc; float v[8];
#pragma unroll
            for (int j = 0; j < 8; ++j) v[j] = rbf(cp[j]);
            const size_t go = (size_t)(m0 + r) * D + n0 + sc;
            u32x4 w;
            if (EPI == 2) { const u32x4 rr = *(const u32x4*)(resid + go);
#pragma unroll
                for (int j = 0; j < 4; ++j) w[j] = cvtpk(v[2 * j] + bf2f((u16)(rr[j] & 0xffff)), v[2 * j + 1] + bf2f((u16)(rr[j] >> 16)));
            } else {
#pragma unroll
                for (int j = 0; j < 4; ++j) w[j] = cvtpk(v[2 * j] * QSCALE, v[2 * j + 1] * QSCALE);
            }
            *(u32x4*)(out + go) = w;
        }
    } else if (EPI == 0) {
        const int c = wn * 32 + r32;
        if (c < 20) {
#pragma unroll
            for (int i = 0; i < 16; ++i) { const int row = m0 + wm * 32 + crow(i, h);
                kimg[row * 32 + (((c >> 3) ^ ((row >> 2) & 3)) << 3) + (c & 7)] = f2bf(acc[i]); }
        } else if (c < 40) {
            const int dim = c - 20, t = (m0 >> 5) + wm;
#pragma unroll
            for (int q2 = 0; q2 < 2; ++q2) { u32x4 w;
#pragma unroll
                for (int j = 0; j < 4; ++j) w[j] = cvtpk(acc[8 * q2 + 2 * j], acc[8 * q2 + 2 * j + 1]);
                *(u32x4*)(vimg + t * 1024 + dim * 32 + (((2 * q2 + h) ^ ((dim >> 2) & 3)) << 3)) = w; }
        }
        for (int e = tid; e < 64 * 12; e += 256) { const int row = m0 + e / 12, c2 = 20 + e % 12;
            kimg[row * 32 + (((c2 >> 3) ^ ((row >> 2) & 3)) << 3) + (c2 & 7)] = 0; }
        for (int e = tid; e < 2 * 12 * 4; e += 256) { const int t = (m0 >> 5) + e / 48, dim = 20 + (e % 48) / 4, ch = e & 3;
            const unsigned val = dim == 20 ? 0x3f803f80u : 0u; u32x4 w = {val, val, val, val};
            *(u32x4*)(vimg + t * 1024 + dim * 32 + ch * 8) = w; }
    } else if (EPI == 1) {
        const int sec = col / D, cc = col - sec * D, hd = cc / DH, dim = cc - hd * DH;
        if (sec == 1) {
            u16* kh = kimg + (size_t)hd * S * 32;
#pragma unroll
            for (int i = 0; i < 16; ++i) { const int row = m0 + wm * 32 + crow(i, h);
                kh[row * 32 + (((dim >> 3) ^ ((row >> 2) & 3)) << 3) + (dim & 7)] = f2bf(acc[i]); }
            if (dim == 19) {
#pragma unroll
                for (int i = 0; i < 16; ++i) { const int row = m0 + wm * 32 + crow(i, h); const int sw = (row >> 2) & 3;
                    *(u32x2*)(kh + row * 32 + ((2 ^ sw) << 3) + 4) = u32x2{0u, 0u};
                    *(u32x4*)(kh + row * 32 + ((3 ^ sw) << 3)) = u32x4{0u, 0u, 0u, 0u}; }
            }
        } else {
            u16* vh = vimg + (size_t)hd * S * 32;
            const int t = (m0 >> 5) + wm;
#pragma unroll
            for (int q2 = 0; q2 < 2; ++q2) { u32x4 w;
#pragma unroll
                for (int j = 0; j < 4; ++j) w[j] = cvtpk(acc[8 * q2 + 2 * j], acc[8 * q2 + 2 * j + 1]);
                *(u32x4*)(vh + t * 1024 + dim * 32 + (((2 * q2 + h) ^ ((dim >> 2) & 3)) << 3)) = w; }
            if (dim == 19) {
#pragma unroll
                for (int d2 = 20; d2 < 32; ++d2) { const unsigned val = d2 == 20 ? 0x3f803f80u : 0u; u32x4 w = {val, val, val, val};
                    *(u32x4*)(vh + t * 1024 + d2 * 32 + 16 * h) = w; *(u32x4*)(vh + t * 1024 + d2 * 32 + 16 * h + 8) = w; }
            }
        }
    }
}

__global__ __launch_bounds__(256) void packkv_kernel(const u16* __restrict__ wk, const u16* __restrict__ wv, u16* __restrict__ dst) {
    const int idx = blockIdx.x * 256 + threadIdx.x, k = idx >> 6, c = idx & 63;
    dst[idx] = c < 20 ? wk[k * 20 + c] : (c < 40 ? wv[k * 20 + c - 20] : (u16)0);
}

struct AttnState { f32x16 o, negm; bool first; };

template <bool MASK>
DI void attn_tile(const char* kt, const char* vt, const bf16x8& q1, const bf16x8& q2, f32x16& o, f32x16& negm, bool& first,
                  int r32, int h, int dq  ) {
    const int sw = (r32 >> 2) & 3;
    const bf16x8 k1 = *(const bf16x8*)(kt + r32 * 64 + ((h ^ sw) << 4));
    const bf16x8 k2 = *(const bf16x8*)(kt + r32 * 64 + (((2 + h) ^ sw) << 4));
    const bf16x8 v1 = *(const bf16x8*)(vt + r32 * 64 + ((h ^ sw) << 4));
    const bf16x8 v2 = *(const bf16x8*)(vt + r32 * 64 + (((2 + h) ^ sw) << 4));
    f32x16 s = MFMA32(k1, q1, negm);
    s = MFMA32(k2, q2, s);
    if (MASK) {
#pragma unroll
        for (int i = 0; i < 16; ++i) { const int c = (i & 3) + 8 * (i >> 2); if ((unsigned)(dq - c) >= (unsigned)WIN) s[i] = -__builtin_inff(); }
    }
    float mx = fmaxf(fmaxf(s[0], s[1]), s[2]);
#pragma unroll
    for (int i = 3; i < 15; i += 2) mx = fmaxf(fmaxf(mx, s[i]), s[i + 1]);
    mx = fmaxf(mx, s[15]);
    if (__builtin_expect(first || __any(mx > THR), 0)) {
        float d = fmaxf(mx, __shfl_xor(mx, 32));
        if (MASK) d = fmaxf(d, -1e30f);
        if (!first) d = fmaxf(d, 0.f);
        const float sc = first ? 1.f : __builtin_amdgcn_exp2f(-d);
#pragma unroll
        for (int i = 0; i < 16; ++i) { s[i] -= d; o[i] *= sc; negm[i] -= d; }
        first = false;
    }
    f32x16 p;
#pragma unroll
    for (int i = 0; i < 16; ++i) p[i] = __builtin_amdgcn_exp2f(s[i]);
    u32x4 w0, w1;
#pragma unroll
    for (int j = 0; j < 4; ++j) { w0[j] = cvtpk(p[2 * j], p[2 * j + 1]); w1[j] = cvtpk(p[8 + 2 * j], p[8 + 2 * j + 1]); }
    o = MFMA32(v1, __builtin_bit_cast(bf16x8, w0), o);
    o = MFMA32(v2, __builtin_bit_cast(bf16x8, w1), o);
}

DI void load_q(const u16* q, int row, int hd, int h, bf16x8& q1, bf16x8& q2) {
    const u16* p = q + (size_t)row * D + hd * DH;
    u32x2 a = *(const u32x2*)(p + 8 * h), b = *(const u32x2*)(p + 8 * h + 4);
    u32x4 w1 = {a[0], a[1], b[0], b[1]};
    u32x2 c = *(const u32x2*)(p + 16);
    u32x4 w2 = {h ? 0u : c[0], h ? 0u : c[1], 0u, 0u};
    q1 = __builtin_bit_cast(bf16x8, w1); q2 = __builtin_bit_cast(bf16x8, w2);
}
DI void store_o(u16* o_out, const f32x16& o, int row, int hd, int h) {
    const float l = __shfl(o[8], (threadIdx.x & 31) + 32);
    const float inv = 1.f / l;
    u16* p = o_out + (size_t)row * D + hd * DH + 4 * h;
#pragma unroll
    for (int g = 0; g < 3; ++g) {
        if (g == 2 && h == 1) break;
        u32x2 w = {cvtpk(o[4 * g] * inv, o[4 * g + 1] * inv), cvtpk(o[4 * g + 2] * inv, o[4 * g + 3] * inv)};
        *(u32x2*)(p + 8 * g) = w;
    }
}

constexpr int CHUNK = 128;
__global__ __launch_bounds__(512, 4) void attnA_kernel(const u16* __restrict__ q, const u16* __restrict__ kimg, const u16* __restrict__ vimg,
                                                        u16* __restrict__ o_out) {
    __shared__ __attribute__((aligned(16))) char lds[2 * 32768];
    const int tid = threadIdx.x, lane = tid & 63, wid = __builtin_amdgcn_readfirstlane(tid >> 6), r32 = lane & 31, h = lane >> 5;
    const int hd = blockIdx.x >> 4, half = wid >> 2, row = (blockIdx.x & 15) * 128 + (wid & 3) * 32 + r32;
    bf16x8 q1, q2; load_q(q, row, hd, h, q1, q2);
    f32x16 o = {}, negm = {}; bool first = true;
    u32x4 st[4];
    auto sload = [&](int c) {
#pragma unroll
        for (int i = 0; i < 2; ++i) { const int off = (i * 1024 + c * CHUNK) * 64 + tid * 16;
            st[2 * i] = *(const u32x4*)((const char*)kimg + off); st[2 * i + 1] = *(const u32x4*)((const char*)vimg + off); } };
    auto sstore = [&](int buf) {
#pragma unroll
        for (int i = 0; i < 2; ++i) { *(u32x4*)(lds + buf * 32768 + i * 16384 + tid * 16) = st[2 * i];
                                      *(u32x4*)(lds + buf * 32768 + i * 16384 + 8192 + tid * 16) = st[2 * i + 1]; } };
    sload(0); sstore(0); __syncthreads();
#pragma unroll 1
    for (int c = 0; c < 1024 / CHUNK; ++c) {
        const int buf = c & 1;
        if (c + 1 < 1024 / CHUNK) sload(c + 1);
        const char* kb = lds + buf * 32768 + half * 16384; const char* vb = kb + 8192;
#pragma unroll
        for (int t = 0; t < CHUNK / 32; ++t) attn_tile<false>(kb + t * 2048, vb + t * 2048, q1, q2, o, negm, first, r32, h, 0);
        if (c + 1 < 1024 / CHUNK) sstore(buf ^ 1);
        __syncthreads();
    }
    float* mb = (float*)lds + (wid & 3) * (17 * 64);
    if (half == 1) {
#pragma unroll
        for (int i = 0; i < 16; ++i) mb[i * 64 + lane] = o[i];
        mb[16 * 64 + lane] = negm[0];
    }
    __syncthreads();
    if (half == 0) {
        const float nm1 = mb[16 * 64 + lane], nm0 = negm[0];
        const float nm = fminf(nm0, nm1);
        const float f0 = __builtin_amdgcn_exp2f(nm - nm0), f1 = __builtin_amdgcn_exp2f(nm - nm1);
#pragma unroll
        for (int i = 0; i < 16; ++i) o[i] = o[i] * f0 + mb[i * 64 + lane] * f1;
        store_o(o_out, o, row, hd, h);
    }
}

__global__ __launch_bounds__(256) void attnB_kernel(const u16* __restrict__ q, const u16* __restrict__ kimg, const u16* __restrict__ vimg,
                                                     u16* __restrict__ o_out) {
    __shared__ __attribute__((aligned(16))) char lds[2 * 384 * 64];
    const int tid = threadIdx.x, lane = tid & 63, wid = __builtin_amdgcn_readfirstlane(tid >> 6), r32 = lane & 31, h = lane >> 5;
    const int hd = blockIdx.x >> 4, q0 = (blockIdx.x & 15) * 128, row = q0 + wid * 32 + r32;
    const int kbase = q0 - 256;
    const char* kg = (const char*)(kimg + (size_t)hd * S * 32); const char* vg = (const char*)(vimg + (size_t)hd * S * 32);
    u32x4 st[12];
#pragma unroll
    for (int i = 0; i < 6; ++i) {
        const int off = i * 4096 + tid * 16;
        const bool ok = kbase * 64 + off >= 0;
        st[2 * i] = ok ? *(const u32x4*)(kg + (ptrdiff_t)kbase * 64 + off) : u32x4{0u, 0u, 0u, 0u};
        st[2 * i + 1] = ok ? *(const u32x4*)(vg + (ptrdiff_t)kbase * 64 + off) : u32x4{0u, 0u, 0u, 0u};
    }
    bf16x8 q1, q2; load_q(q, row, hd, h, q1, q2);
#pragma unroll
    for (int i = 0; i < 6; ++i) { const int off = i * 4096 + tid * 16;
        *(u32x4*)(lds + off) = st[2 * i]; *(u32x4*)(lds + 24576 + off) = st[2 * i + 1]; }
    f32x16 o = {}, negm = {}; bool first = true;
    __syncthreads();
    const int lo = max(wid, (-kbase) >> 5);
    {
        const int lt = wid + 8;
        attn_tile<true>(lds + lt * 2048, lds + 24576 + lt * 2048, q1, q2, o, negm, first, r32, h, row - (kbase + lt * 32) - 4 * h);
    }
#pragma unroll 1
    for (int lt = wid + 7; lt > wid && lt >= lo; --lt)
        attn_tile<false>(lds + lt * 2048, lds + 24576 + lt * 2048, q1, q2, o, negm, first, r32, h, 0);
    if (wid >= lo)
        attn_tile<true>(lds + wid * 2048, lds + 24576 + wid * 2048, q1, q2, o, negm, first, r32, h, row - (kbase + wid * 32) - 4 * h);
    store_o(o_out, o, row, hd, h);
}

extern "C" void kernel_launch(void* const* d_in, const int* in_sizes, int n_in, void* d_out, int out_size, void* d_ws,
                              size_t ws_size, hipStream_t stream) {
    const u16* Ax = (const u16*)d_in[0]; const u16* Awq = (const u16*)d_in[1]; const u16* Awk = (const u16*)d_in[2];
    const u16* Awv = (const u16*)d_in[3]; const u16* Awo = (const u16*)d_in[4];
    const u16* Bwqkv = (const u16*)d_in[6]; const u16* Bwo = (const u16*)d_in[7];
    char* ws = (char*)d_ws;
    const size_t XB = (size_t)S * D * 2;
    u16* x1 = (u16*)(ws); u16* x2 = (u16*)(ws + XB); u16* x3 = (u16*)(ws + 2 * XB);
    u16* qb = (u16*)(ws + 3 * XB); u16* ob = (u16*)(ws + 4 * XB);
    u16* kA = (u16*)(ws + 5 * XB); u16* vA = kA + S * 32;
    u16* kB = (u16*)(ws + 6 * XB); u16* vB = kB + (size_t)NH * S * 32;
    u16* out = (u16*)d_out;
    u16* wkv = (u16*)(ws + 5 * XB + (1 << 20));
    packkv_kernel<<<D * 64 / 256, 256, 0, stream>>>(Awk, Awv, wkv);
    auto layerA = [&](const u16* xin, u16* xout) {
        gemm_kernel<true, 0><<<dim3(11, 32), 256, 0, stream>>>(xin, Awq, wkv, nullptr, D, nullptr, qb, kA, vA);
        attnA_kernel<<<512, 512, 0, stream>>>(qb, kA, vA, ob);
        gemm_kernel<false, 2><<<dim3(10, 32), 256, 0, stream>>>(ob, Awo, nullptr, nullptr, D, xin, xout, nullptr, nullptr);
    };
    auto layerB = [&](const u16* xin, u16* xout) {
        gemm_kernel<true, 1><<<dim3(30, 32), 256, 0, stream>>>(xin, Bwqkv, nullptr, nullptr, 3 * D, nullptr, qb, kB, vB);
        attnB_kernel<<<512, 256, 0, stream>>>(qb, kB, vB, ob);
        gemm_kernel<false, 2><<<dim3(10, 32), 256, 0, stream>>>(ob, Bwo, nullptr, nullptr, D, xin, xout, nullptr, nullptr);
    };
    layerA(Ax, x1); layerB(x1, x2); layerA(x2, x3); layerB(x3, out);
}
```
